```python
import torch

def _softmax(x, axis=-1):
    return torch.softmax(x, dim=axis)
def _gelu(x):
    return torch.nn.functional.gelu(x, approximate="tanh")
def _silu(x):
    return torch.nn.functional.silu(x)
def _sigmoid(x):
    return torch.sigmoid(x)
def _layernorm(x, axis=-1, eps=1e-5):
    m = x.mean(dim=axis, keepdim=True)
    v = x.var(dim=axis, keepdim=True, unbiased=False)
    return (x - m) / torch.sqrt(v + eps)
def _scatter_add(tgt, idx, src, axis=0):
    return tgt.clone().index_add_(axis, idx, src)
def _segment_sum(src, seg, n):
    out = torch.zeros((n,) + src.shape[1:], dtype=src.dtype)
    return out.index_add_(0, seg, src)
def _pad(x, pw):
    flat = tuple(v for pair in reversed(pw) for v in pair)
    return torch.nn.functional.pad(x, flat)

kernel_name = 'sequential_preLN__linear_kernel_hedgehog__swiglu_ffn__prompt'

def setup_inputs():

    _w = (lambda a: (lambda t: t.to(torch.bfloat16) if t.is_floating_point() else t)(torch.as_tensor(a)))
    def _A_setup():
        B, S, D, H = 1, 2048, 512, 32
        Dh = D // H
        return {
            "x": _w((lambda *s: torch.randn(*s, dtype=torch.bfloat16) * 0.05)(B, S, D)),
            "wqkv": _w((lambda *s: torch.randn(*s, dtype=torch.bfloat16) * 0.2)(D, 3 * D)),
            "w_phi": _w((lambda *s: torch.randn(*s, dtype=torch.bfloat16) * 0.2)(H, Dh, Dh) * (Dh ** -0.5)),
            "wo": _w((lambda *s: torch.randn(*s, dtype=torch.bfloat16) * 0.05)(D, D)),
        }
    _A_in = _A_setup()

    def _B_setup():
        B, S, D, F = 1, 2048, 512, 1792
        return {
            "x": _w((lambda *s: torch.randn(*s, dtype=torch.bfloat16) * 0.05)(B, S, D)),
            "w_gate": _w((lambda *s: torch.randn(*s, dtype=torch.bfloat16) * 0.05)(D, F)),
            "w_up": _w((lambda *s: torch.randn(*s, dtype=torch.bfloat16) * 0.05)(D, F)),
            "w_down": _w((lambda *s: torch.randn(*s, dtype=torch.bfloat16) * 0.05)(F, D)),
        }
    _B_in = _B_setup()

    return {
        "A_x": _A_in["x"],
        "A_wqkv": _A_in["wqkv"],
        "A_w_phi": _A_in["w_phi"],
        "A_wo": _A_in["wo"],
        "B_x": _B_in["x"],
        "B_w_gate": _B_in["w_gate"],
        "B_w_up": _B_in["w_up"],
        "B_w_down": _B_in["w_down"],
    }

def reference(A_x, A_wqkv, A_w_phi, A_wo, B_x, B_w_gate, B_w_up, B_w_down):
    def _A_ref(x, wqkv, w_phi, wo):
        B, S, D = x.shape
        H = 32; Dh = D // H
        qkv = (lambda x, *d: x.permute(*d))((x @ wqkv).reshape(B, S, 3, H, Dh), 2, 0, 3, 1, 4)
        q, k, v = qkv[0], qkv[1], qkv[2]
        pq = torch.einsum("bhsd,hdf->bhsf", q, w_phi)
        pk = torch.einsum("bhsd,hdf->bhsf", k, w_phi)
        phi_q = torch.concatenate([_softmax(pq, axis=-1), _softmax(-pq, axis=-1)], axis=-1)
        phi_k = torch.concatenate([_softmax(pk, axis=-1), _softmax(-pk, axis=-1)], axis=-1)
        kv = torch.einsum("bhsf,bhsd->bhfd", phi_k, v)
        z  = torch.einsum("bhsf->bhf", phi_k)
        num = torch.einsum("bhqf,bhfd->bhqd", phi_q, kv)
        den = torch.einsum("bhqf,bhf->bhq", phi_q, z)[..., None] + 1e-6
        o = num / den
        return (lambda x, *d: x.permute(*d))(o, 0, 2, 1, 3).reshape(B, S, D) @ wo

    def _B_ref(x, w_gate, w_up, w_down):
        g = _silu(x @ w_gate)
        u = x @ w_up
        return (g * u) @ w_down

    x = A_x
    h = x + _A_ref(_layernorm(x, axis=-1), A_wqkv, A_w_phi, A_wo)
    h2 = h + _B_ref(_layernorm(h, axis=-1), B_w_gate, B_w_up, B_w_down)
    return h2
```

```cpp
#include <hip/hip_runtime.h>
#include <hip/hip_bf16.h>
#include <stdint.h>


typedef __attribute__((ext_vector_type(8))) __bf16 bf16x8;
typedef __attribute__((ext_vector_type(4))) short s16x4;
typedef __attribute__((ext_vector_type(4))) float f32x4;
typedef __attribute__((ext_vector_type(4))) unsigned u32x4;
typedef __attribute__((ext_vector_type(2))) unsigned u32x2;
typedef unsigned short u16;

#define S_TOK 2048
#define D_MODEL 512
#define N_HEAD 32
#define F_DIM 1792

#define LDS_AS __attribute__((address_space(3)))

__device__ __forceinline__ float bf2f(unsigned b) { return __uint_as_float(b << 16); }
__device__ __forceinline__ unsigned f2bf(float f) {
  __bf16 h = (__bf16)f;
  return (unsigned)__builtin_bit_cast(unsigned short, h);
}
__device__ __forceinline__ float rbf(float f) { return bf2f(f2bf(f)); }
__device__ __forceinline__ unsigned pack2(float lo, float hi) { return f2bf(lo) | (f2bf(hi) << 16); }

__device__ __forceinline__ f32x4 mfma32(bf16x8 a, bf16x8 b, f32x4 c) {
  return __builtin_amdgcn_mfma_f32_16x16x32_bf16(a, b, c, 0, 0, 0);
}
__device__ __forceinline__ f32x4 mfma16(s16x4 a, s16x4 b, f32x4 c) {
  return __builtin_amdgcn_mfma_f32_16x16x16bf16_1k(a, b, c, 0, 0, 0);
}
__device__ __forceinline__ s16x4 tr_read(const char* p) {
  return __builtin_amdgcn_ds_read_tr16_b64_v4i16((LDS_AS s16x4*)(p));
}
__device__ __forceinline__ void glds16(const void* g, char* l) {
  __builtin_amdgcn_global_load_lds((const __attribute__((address_space(1))) unsigned*)g, (LDS_AS unsigned*)l, 16, 0, 0);
}
#define WAIT_VM(n) asm volatile("s_waitcnt vmcnt(" #n ")" ::: "memory")
#define RAW_BARRIER() do { asm volatile("s_waitcnt lgkmcnt(0)" ::: "memory"); __builtin_amdgcn_s_barrier(); } while (0)

__device__ __forceinline__ float wsum_16_32(float v) {
  v += __shfl_xor(v, 16);
  v += __shfl_xor(v, 32);
  return v;
}
__device__ __forceinline__ float wmax_16_32(float v) {
  v = fmaxf(v, __shfl_xor(v, 16));
  v = fmaxf(v, __shfl_xor(v, 32));
  return v;
}
__device__ __forceinline__ float wsum16(float v) {
  v += __shfl_xor(v, 1);
  v += __shfl_xor(v, 2);
  v += __shfl_xor(v, 4);
  v += __shfl_xor(v, 8);
  return v;
}
__device__ __forceinline__ float wmax16(float v) {
  v = fmaxf(v, __shfl_xor(v, 1));
  v = fmaxf(v, __shfl_xor(v, 2));
  v = fmaxf(v, __shfl_xor(v, 4));
  v = fmaxf(v, __shfl_xor(v, 8));
  return v;
}

__global__ __launch_bounds__(512) void k1_qkv(const u16* __restrict__ x, const u16* __restrict__ wqkv,
                                              const u16* __restrict__ wphi, u16* __restrict__ phiq,
                                              float* __restrict__ kvpart, float* __restrict__ zpart) {
  __shared__ __attribute__((aligned(16))) char smem[163840];
  char* As = smem;
  char* Ws = smem + 65536;
  const int tid = threadIdx.x, wave = tid >> 6, lane = tid & 63;
  const int b = blockIdx.x, xcd = b & 7, idx = b >> 3;
  const int rt = xcd * 2 + (idx >> 4), hp = idx & 15;
  const int row0 = rt * 128;

#pragma unroll
  for (int j = 0; j < 12; ++j) {
    int ii = wave * 12 + j;
    int c = ii * 64 + lane;
    int cqs = c & 3, kr = (c >> 2) & 7, sub = c >> 5;
    int nb = sub % 3, kb = sub / 3;
    int cq = cqs ^ ((kb & 1) << 1);
    const u16* g = wqkv + (size_t)(kb * 8 + kr) * 1536 + nb * 512 + hp * 32 + cq * 8;
    glds16(g, Ws + ii * 1024);
  }
  u32x4 xr[2][8];
#pragma unroll
  for (int p = 0; p < 2; ++p)
#pragma unroll
    for (int i = 0; i < 8; ++i)
      xr[p][i] = *(const u32x4*)(x + (size_t)(row0 + wave * 16 + p * 8 + (lane >> 3)) * 512 + i * 64 + (lane & 7) * 8);

  const int g4 = lane >> 4, l16 = lane & 15;
  const int wn = wave & 1, wm = wave >> 1;
  const int head = hp * 2 + wn;
  s16x4 wph;
#pragma unroll
  for (int j = 0; j < 4; ++j) wph[j] = (short)wphi[head * 256 + (g4 * 4 + j) * 16 + l16];

  float mean[2], rden[2];
#pragma unroll
  for (int p = 0; p < 2; ++p) {
    float s = 0.f;
#pragma unroll
    for (int i = 0; i < 8; ++i)
#pragma unroll
      for (int e = 0; e < 4; ++e) s += bf2f(xr[p][i][e] & 0xffff) + bf2f(xr[p][i][e] >> 16);
    s += __shfl_xor(s, 1); s += __shfl_xor(s, 2); s += __shfl_xor(s, 4);
    float m = s * (1.0f / 512.0f);
    float q = 0.f;
#pragma unroll
    for (int i = 0; i < 8; ++i)
#pragma unroll
      for (int e = 0; e < 4; ++e) {
        float a = bf2f(xr[p][i][e] & 0xffff) - m, c = bf2f(xr[p][i][e] >> 16) - m;
        q += a * a + c * c;
      }
    q += __shfl_xor(q, 1); q += __shfl_xor(q, 2); q += __shfl_xor(q, 4);
    float var = rbf(q * (1.0f / 512.0f));
    float den = rbf(sqrtf(rbf(var + 1e-5f)));
    mean[p] = rbf(m);
    rden[p] = den;
  }
  auto write_half = [&](int half) {
#pragma unroll
    for (int p = 0; p < 2; ++p) {
      int r = wave * 16 + p * 8 + (lane >> 3);
#pragma unroll
      for (int i = 0; i < 4; ++i) {
        u32x4 v = xr[p][half * 4 + i], o;
#pragma unroll
        for (int e = 0; e < 4; ++e) {
          float a = rbf(bf2f(v[e] & 0xffff) - mean[p]) / rden[p];
          float c = rbf(bf2f(v[e] >> 16) - mean[p]) / rden[p];
          o[e] = pack2(a, c);
        }
        int chunk = i * 8 + (lane & 7);
        *(u32x4*)(As + r * 512 + ((chunk ^ (r & 15)) << 4)) = o;
      }
    }
  };

  f32x4 acc[2][3];
#pragma unroll
  for (int mi = 0; mi < 2; ++mi)
#pragma unroll
    for (int t = 0; t < 3; ++t) acc[mi][t] = (f32x4){0.f, 0.f, 0.f, 0.f};

  const int tq = l16 >> 2, tp = l16 & 3;
  auto kloop = [&](int half) {
#pragma unroll
    for (int ks = 0; ks < 8; ++ks) {
      bf16x8 af[2], wf[3];
#pragma unroll
      for (int mi = 0; mi < 2; ++mi) {
        int r = wm * 32 + mi * 16 + l16;
        int chunk = ks * 4 + g4;
        af[mi] = *(const bf16x8*)(As + r * 512 + ((chunk ^ (r & 15)) << 4));
      }
      int kb = (half * 8 + ks) * 4 + g4;
#pragma unroll
      for (int t = 0; t < 3; ++t) {
        int cq = (wn * 2 + (tp >> 1)) ^ ((g4 & 1) << 1);
        const char* p0 = Ws + ((kb * 3 + t) * 8 + tq) * 64 + cq * 16 + (tp & 1) * 8;
        s16x4 lo = tr_read(p0), hi = tr_read(p0 + 256);
        u32x4 w = {((unsigned*)&lo)[0], ((unsigned*)&lo)[1], ((unsigned*)&hi)[0], ((unsigned*)&hi)[1]};
        wf[t] = __builtin_bit_cast(bf16x8, w);
      }
#pragma unroll
      for (int mi = 0; mi < 2; ++mi) {
        acc[mi][0] = mfma32(wf[0], af[mi], acc[mi][0]);
        acc[mi][1] = mfma32(wf[1], af[mi], acc[mi][1]);
        acc[mi][2] = mfma32(af[mi], wf[2], acc[mi][2]);
      }
    }
  };

  write_half(0);
  __syncthreads();
  kloop(0);
  __syncthreads();
  write_half(1);
  __syncthreads();
  kloop(1);

  f32x4 kvacc[2] = {{0.f, 0.f, 0.f, 0.f}, {0.f, 0.f, 0.f, 0.f}};
  float zp[2] = {0.f, 0.f};
#pragma unroll
  for (int mi = 0; mi < 2; ++mi) {
    s16x4 qb, kbf, vb;
#pragma unroll
    for (int r = 0; r < 4; ++r) {
      qb[r] = (short)f2bf(acc[mi][0][r]);
      kbf[r] = (short)f2bf(acc[mi][1][r]);
      vb[r] = (short)f2bf(acc[mi][2][r]);
    }
    f32x4 zero = {0.f, 0.f, 0.f, 0.f};
    f32x4 pq = mfma16(wph, qb, zero);
    f32x4 pk = mfma16(kbf, wph, zero);
    {
      float v[4], mx = -3.0e38f, mn = 3.0e38f;
#pragma unroll
      for (int r = 0; r < 4; ++r) { v[r] = rbf(pq[r]); mx = fmaxf(mx, v[r]); mn = fminf(mn, v[r]); }
      mx = wmax_16_32(mx);
      mn = -wmax_16_32(-mn);
      float ep[4], en[4], sp = 0.f, sn = 0.f;
#pragma unroll
      for (int r = 0; r < 4; ++r) { ep[r] = __expf(v[r] - mx); en[r] = __expf(mn - v[r]); sp += ep[r]; sn += en[r]; }
      sp = wsum_16_32(sp);
      sn = wsum_16_32(sn);
      float ip = 1.0f / sp, in_ = 1.0f / sn;
      int token = row0 + wm * 32 + mi * 16 + l16;
      u16* dst = phiq + (size_t)token * 1024 + head * 32 + g4 * 4;
      u32x2 o1 = {pack2(ep[0] * ip, ep[1] * ip), pack2(ep[2] * ip, ep[3] * ip)};
      u32x2 o2 = {pack2(en[0] * in_, en[1] * in_), pack2(en[2] * in_, en[3] * in_)};
      *(u32x2*)dst = o1;
      *(u32x2*)(dst + 16) = o2;
    }
    {
      s16x4 php, phn;
#pragma unroll
      for (int r = 0; r < 4; ++r) {
        float v = rbf(pk[r]);
        float mx = wmax16(v), mn = -wmax16(-v);
        float ep = __expf(v - mx), en = __expf(mn - v);
        float sp = wsum16(ep), sn = wsum16(en);
        unsigned a = f2bf(ep / sp), c = f2bf(en / sn);
        php[r] = (short)a;
        phn[r] = (short)c;
        zp[0] += bf2f(a);
        zp[1] += bf2f(c);
      }
      kvacc[0] = mfma16(php, vb, kvacc[0]);
      kvacc[1] = mfma16(phn, vb, kvacc[1]);
    }
  }
  zp[0] = wsum_16_32(zp[0]);
  zp[1] = wsum_16_32(zp[1]);
  __syncthreads();
  float* red = (float*)smem;
  float* redz = red + 8 * 512;
#pragma unroll
  for (int sg = 0; sg < 2; ++sg) *(f32x4*)(red + wave * 512 + sg * 256 + l16 * 16 + g4 * 4) = kvacc[sg];
  if (g4 == 0) { redz[wave * 32 + l16] = zp[0]; redz[wave * 32 + 16 + l16] = zp[1]; }
  __syncthreads();
  {
    int hh = tid >> 8, e = tid & 255;
    int sg = e >> 7, d = (e >> 3) & 15, fq = e & 7;
    float s0 = 0.f, s1 = 0.f;
#pragma unroll
    for (int w = 0; w < 4; ++w) {
      const float* p = red + (w * 2 + hh) * 512 + sg * 256 + d * 16 + fq * 2;
      s0 += p[0];
      s1 += p[1];
    }
    float* dst = kvpart + ((size_t)(rt * 32 + hp * 2 + hh) * 16 + d) * 32 + sg * 16 + fq * 2;
    dst[0] = s0;
    dst[1] = s1;
    if (tid < 64) {
      int h2 = tid >> 5, f = tid & 31;
      float z = 0.f;
#pragma unroll
      for (int w = 0; w < 4; ++w) z += redz[(w * 2 + h2) * 32 + f];
      zpart[(rt * 32 + hp * 2 + h2) * 32 + f] = z;
    }
  }
}

__global__ __launch_bounds__(256) void k2_attn(const u16* __restrict__ phiq, const float* __restrict__ kvpart,
                                               const float* __restrict__ zpart, u16* __restrict__ o,
                                               const u16* __restrict__ wg, const u16* __restrict__ wu,
                                               float* __restrict__ colsum) {
  __shared__ __attribute__((aligned(16))) float sm[2048 + 64];
  const int tid = threadIdx.x, wave = tid >> 6, lane = tid & 63;
  const int b = blockIdx.x;
  if (b >= 256) {
    int j = b - 256;
    const u16* w = (j < 14) ? wg : wu;
    int c0 = (j % 14) * 128;
    int ch = tid & 15, rg = tid >> 4;
    float s[8] = {0.f, 0.f, 0.f, 0.f, 0.f, 0.f, 0.f, 0.f};
#pragma unroll 8
    for (int r = 0; r < 32; ++r) {
      u32x4 v = *(const u32x4*)(w + (size_t)(rg * 32 + r) * F_DIM + c0 + ch * 8);
#pragma unroll
      for (int e = 0; e < 4; ++e) { s[e * 2] += bf2f(v[e] & 0xffff); s[e * 2 + 1] += bf2f(v[e] >> 16); }
    }
#pragma unroll
    for (int e = 0; e < 8; ++e) sm[rg * 128 + ch * 8 + e] = s[e];
    __syncthreads();
    if (tid < 128) {
      float t = 0.f;
#pragma unroll
      for (int r = 0; r < 16; ++r) t += sm[r * 128 + tid];
      colsum[(j < 14 ? 0 : F_DIM) + c0 + tid] = t;
    }
    return;
  }
  const int xcd = b & 7, idx = b >> 3;
  const int rt = xcd * 2 + (idx >> 4), hp = idx & 15;
  const int row0 = rt * 128;
  u16* kvs = (u16*)sm;
  u16* zs = kvs + 1024;
  {
    int e = tid * 4;
    int hh = e >> 9, r = e & 511;
    f32x4 s = {0.f, 0.f, 0.f, 0.f};
#pragma unroll
    for (int p = 0; p < 16; ++p) s += *(const f32x4*)(kvpart + (size_t)(p * 32 + hp * 2 + hh) * 512 + r);
    u32x2 v = {pack2(s[0], s[1]), pack2(s[2], s[3])};
    *(u32x2*)(kvs + e) = v;
    if (tid < 64) {
      float z = 0.f;
#pragma unroll
      for (int p = 0; p < 16; ++p) z += zpart[(p * 32 + hp * 2 + (tid >> 5)) * 32 + (tid & 31)];
      zs[tid] = (u16)f2bf(z);
    }
  }
  __syncthreads();
  const int g4 = lane >> 4, l16 = lane & 15;
#pragma unroll
  for (int hh = 0; hh < 2; ++hh) {
    bf16x8 akv = *(const bf16x8*)(kvs + hh * 512 + l16 * 32 + g4 * 8);
    bf16x8 az = *(const bf16x8*)(zs + hh * 32 + g4 * 8);
    int head = hp * 2 + hh;
#pragma unroll
    for (int mi = 0; mi < 2; ++mi) {
      int token = row0 + wave * 32 + mi * 16 + l16;
      bf16x8 pf = *(const bf16x8*)(phiq + (size_t)token * 1024 + head * 32 + g4 * 8);
      f32x4 zero = {0.f, 0.f, 0.f, 0.f};
      f32x4 num = mfma32(akv, pf, zero);
      f32x4 den = mfma32(az, pf, zero);
      float ov[4];
#pragma unroll
      for (int r = 0; r < 4; ++r) {
        float dn = rbf(rbf(den[r]) + 1e-6f);
        ov[r] = rbf(num[r]) / dn;
      }
      u32x2 v = {pack2(ov[0], ov[1]), pack2(ov[2], ov[3])};
      *(u32x2*)(o + (size_t)token * 512 + head * 16 + g4 * 4) = v;
    }
  }
}

struct GemmArgs {
  const u16* A; const u16* W; const u16* W2; const u16* resid; u16* out; float* stats; const float* colsum;
};

template <int BM, int BN, int WM, int WN, int NK, int NST, int MODE>
__global__ __launch_bounds__(WM* WN * 64) void gemm_k(GemmArgs args) {
  constexpr int NW = WM * WN, WTM = BM / WM, WTN = BN / WN, MT = WTM / 16, NT = WTN / 16, NB = BN / 32;
  constexpr int ASZ = BM * 128, WSZ = BN * 128, STG = ASZ + WSZ;
  constexpr int APER = BM / 8 / NW, WPER = BN / 8 / NW, PER = APER + WPER;
  constexpr int LDA = (MODE == 5) ? F_DIM : 512;
  constexpr int LDW = (MODE == 4) ? F_DIM : 512;
  constexpr int NCT = (MODE == 4) ? (F_DIM / (BN / 2)) : (512 / BN);
  constexpr int NRT = S_TOK / BM, RPX = NRT / 8;
  __shared__ __attribute__((aligned(16))) char smem[NST * STG];
  const int tid = threadIdx.x, wave = tid >> 6, lane = tid & 63;
  const int b = blockIdx.x, xcd = b & 7, idx = b >> 3;
  const int rt = xcd * RPX + idx / NCT, ct = idx % NCT;
  const int row0 = rt * BM;
  const int g4 = lane >> 4, l16 = lane & 15;
  const int wm = wave / WN, wn = wave % WN;

  const u16* asrc[APER];
  const u16* wsrc[WPER];
#pragma unroll
  for (int j = 0; j < APER; ++j) {
    int ii = wave * APER + j;
    int r = ii * 8 + (lane >> 3);
    int chunk = (lane & 7) ^ (r & 7);
    asrc[j] = args.A + (size_t)(row0 + r) * LDA + chunk * 8;
  }
#pragma unroll
  for (int j = 0; j < WPER; ++j) {
    int ii = wave * WPER + j;
    int c = ii * 64 + lane;
    int cqs = c & 3, kr = (c >> 2) & 7, sub = c >> 5;
    int nb = sub % NB, kb = sub / NB;
    int cq = cqs ^ ((kb & 1) << 1);
    if constexpr (MODE == 4) {
      const u16* base = (nb & 1) ? args.W2 : args.W;
      wsrc[j] = base + (size_t)(kb * 8 + kr) * LDW + ct * (BN / 2) + (nb >> 1) * 32 + cq * 8;
    } else {
      wsrc[j] = args.W + (size_t)(kb * 8 + kr) * LDW + ct * BN + nb * 32 + cq * 8;
    }
  }
  auto issue = [&](int kt) {
    char* st = smem + (kt % NST) * STG;
#pragma unroll
    for (int j = 0; j < APER; ++j) glds16(asrc[j] + kt * 64, st + (wave * APER + j) * 1024);
#pragma unroll
    for (int j = 0; j < WPER; ++j) glds16(wsrc[j] + (size_t)kt * 64 * LDW, st + ASZ + (wave * WPER + j) * 1024);
  };

  f32x4 acc[MT][NT];
#pragma unroll
  for (int mi = 0; mi < MT; ++mi)
#pragma unroll
    for (int ni = 0; ni < NT; ++ni) acc[mi][ni] = (f32x4){0.f, 0.f, 0.f, 0.f};

#pragma unroll
  for (int s = 0; s < NST - 1; ++s)
    if (s < NK) issue(s);

  const int tq = l16 >> 2, tp = l16 & 3;
  for (int kt = 0; kt < NK; ++kt) {
    if (NK - 1 - kt >= NST - 2) {
      asm volatile("s_waitcnt vmcnt(%0)" ::"n"(PER * (NST - 2)) : "memory");
    } else {
      asm volatile("s_waitcnt vmcnt(0)" ::: "memory");
    }
    RAW_BARRIER();
    if (kt + NST - 1 < NK) issue(kt + NST - 1);
    const char* st = smem + (kt % NST) * STG;
#pragma unroll
    for (int ks = 0; ks < 2; ++ks) {
      bf16x8 af[MT], wf[NT];
#pragma unroll
      for (int mi = 0; mi < MT; ++mi) {
        int r = wm * WTM + mi * 16 + l16;
        af[mi] = *(const bf16x8*)(st + r * 128 + (((ks * 4 + g4) ^ (r & 7)) << 4));
      }
#pragma unroll
      for (int ni = 0; ni < NT; ++ni) {
        int n = wn * WTN + ni * 16;
        int kb = ks * 4 + g4;
        int cq = (((n >> 4) & 1) * 2 + (tp >> 1)) ^ ((g4 & 1) << 1);
        const char* p0 = st + ASZ + ((kb * NB + (n >> 5)) * 8 + tq) * 64 + cq * 16 + (tp & 1) * 8;
        s16x4 lo = tr_read(p0), hi = tr_read(p0 + 256);
        u32x4 w = {((unsigned*)&lo)[0], ((unsigned*)&lo)[1], ((unsigned*)&hi)[0], ((unsigned*)&hi)[1]};
        wf[ni] = __builtin_bit_cast(bf16x8, w);
      }
#pragma unroll
      for (int mi = 0; mi < MT; ++mi)
#pragma unroll
        for (int ni = 0; ni < NT; ++ni) acc[mi][ni] = mfma32(wf[ni], af[mi], acc[mi][ni]);
    }
  }

  if constexpr (MODE == 3) {
#pragma unroll
    for (int mi = 0; mi < MT; ++mi) {
      int token = row0 + wm * WTM + mi * 16 + l16;
      float hv[NT][4];
      float s1 = 0.f;
#pragma unroll
      for (int ni = 0; ni < NT; ++ni) {
        int n = ct * BN + wn * WTN + ni * 16 + g4 * 4;
        u32x2 xv = *(const u32x2*)(args.resid + (size_t)token * 512 + n);
        hv[ni][0] = rbf(bf2f(xv[0] & 0xffff) + rbf(acc[mi][ni][0]));
        hv[ni][1] = rbf(bf2f(xv[0] >> 16) + rbf(acc[mi][ni][1]));
        hv[ni][2] = rbf(bf2f(xv[1] & 0xffff) + rbf(acc[mi][ni][2]));
        hv[ni][3] = rbf(bf2f(xv[1] >> 16) + rbf(acc[mi][ni][3]));
        u32x2 ov = {pack2(hv[ni][0], hv[ni][1]), pack2(hv[ni][2], hv[ni][3])};
        *(u32x2*)(args.out + (size_t)token * 512 + n) = ov;
        s1 += hv[ni][0] + hv[ni][1] + hv[ni][2] + hv[ni][3];
      }
      s1 = wsum_16_32(s1);
      float mloc = s1 * (1.0f / (float)WTN);
      float m2 = 0.f;
#pragma unroll
      for (int ni = 0; ni < NT; ++ni)
#pragma unroll
        for (int r = 0; r < 4; ++r) { float d = hv[ni][r] - mloc; m2 += d * d; }
      m2 = wsum_16_32(m2);
      if (g4 == 0) {
        float2 sv = make_float2(s1, m2);
        *(float2*)(args.stats + ((size_t)token * (512 / WTN) + (ct * BN + wn * WTN) / WTN) * 2) = sv;
      }
    }
  } else if constexpr (MODE == 4) {
    constexpr int NPART = 16;
    float* rs = (float*)smem;
    RAW_BARRIER();
    if (tid < BM) {
      const float2* sp = (const float2*)(args.stats) + (size_t)(row0 + tid) * NPART;
      float s1 = 0.f;
      float2 pv[NPART];
#pragma unroll
      for (int p = 0; p < NPART; ++p) { pv[p] = sp[p]; s1 += pv[p].x; }
      float mean = s1 * (1.0f / 512.0f);
      float m2 = 0.f;
#pragma unroll
      for (int p = 0; p < NPART; ++p) { float d = pv[p].x * (1.0f / 32.0f) - mean; m2 += pv[p].y + 32.0f * d * d; }
      float var = rbf(m2 * (1.0f / 512.0f));
      float den = rbf(sqrtf(rbf(var + 1e-5f)));
      rs[tid * 2] = rbf(mean);
      rs[tid * 2 + 1] = 1.0f / den;
    }
    __syncthreads();
#pragma unroll
    for (int mi = 0; mi < MT; ++mi) {
      int tl = wm * WTM + mi * 16 + l16;
      float mean = rs[tl * 2], rstd = rs[tl * 2 + 1];
      int token = row0 + tl;
#pragma unroll
      for (int ni = 0; ni < NT / 2; ++ni) {
        int f = ct * (BN / 2) + wn * 32 + ni * 16 + g4 * 4;
        f32x4 cg = *(const f32x4*)(args.colsum + f);
        f32x4 cu = *(const f32x4*)(args.colsum + F_DIM + f);
        float ov[4];
#pragma unroll
        for (int r = 0; r < 4; ++r) {
          float gv = rbf((acc[mi][ni][r] - mean * cg[r]) * rstd);
          float uv = rbf((acc[mi][ni + NT / 2][r] - mean * cu[r]) * rstd);
          float sg = rbf(gv / (1.0f + __expf(-gv)));
          ov[r] = sg * uv;
        }
        u32x2 o2 = {pack2(ov[0], ov[1]), pack2(ov[2], ov[3])};
        *(u32x2*)(args.out + (size_t)token * F_DIM + f) = o2;
      }
    }
  } else {
#pragma unroll
    for (int mi = 0; mi < MT; ++mi) {
      int token = row0 + wm * WTM + mi * 16 + l16;
#pragma unroll
      for (int ni = 0; ni < NT; ++ni) {
        int n = ct * BN + wn * WTN + ni * 16 + g4 * 4;
        u32x2 hv = *(const u32x2*)(args.resid + (size_t)token * 512 + n);
        float o0 = bf2f(hv[0] & 0xffff) + rbf(acc[mi][ni][0]);
        float o1 = bf2f(hv[0] >> 16) + rbf(acc[mi][ni][1]);
        float o2 = bf2f(hv[1] & 0xffff) + rbf(acc[mi][ni][2]);
        float o3 = bf2f(hv[1] >> 16) + rbf(acc[mi][ni][3]);
        u32x2 ov = {pack2(o0, o1), pack2(o2, o3)};
        *(u32x2*)(args.out + (size_t)token * 512 + n) = ov;
      }
    }
  }
}

extern "C" void kernel_launch(void* const* d_in, const int* in_sizes, int n_in, void* d_out, int out_size, void* d_ws,
                              size_t ws_size, hipStream_t stream) {
  const u16* x = (const u16*)d_in[0];
  const u16* wqkv = (const u16*)d_in[1];
  const u16* wphi = (const u16*)d_in[2];
  const u16* wo = (const u16*)d_in[3];
  const u16* wg = (const u16*)d_in[5];
  const u16* wu = (const u16*)d_in[6];
  const u16* wd = (const u16*)d_in[7];
  u16* out = (u16*)d_out;

  char* ws = (char*)d_ws;
  u16* phiq = (u16*)(ws);
  float* kvpart = (float*)(ws + (4u << 20));
  float* zpart = (float*)(ws + (5u << 20));
  float* colsum = (float*)(ws + (5u << 20) + 65536);
  float* stats = (float*)(ws + (5u << 20) + 131072);
  u16* o = (u16*)(ws + (6u << 20));
  u16* h = (u16*)(ws + (8u << 20));
  u16* act = (u16*)(ws + (10u << 20));

  k1_qkv<<<256, 512, 0, stream>>>(x, wqkv, wphi, phiq, kvpart, zpart);
  k2_attn<<<256 + 28, 256, 0, stream>>>(phiq, kvpart, zpart, o, wg, wu, colsum);
  GemmArgs a3 = {o, wo, nullptr, x, h, stats, nullptr};
  gemm_k<64, 64, 2, 2, 8, 8, 3><<<256, 256, 0, stream>>>(a3);
  GemmArgs a4 = {h, wg, wu, nullptr, act, stats, colsum};
  gemm_k<128, 256, 2, 4, 8, 3, 4><<<224, 512, 0, stream>>>(a4);
  GemmArgs a5 = {act, wd, nullptr, h, out, nullptr, nullptr};
  gemm_k<64, 64, 2, 2, 28, 8, 5><<<256, 256, 0, stream>>>(a5);
}
```

```cpp
#include <hip/hip_runtime.h>
#include <hip/hip_bf16.h>
#include <stdint.h>


typedef __attribute__((ext_vector_type(8))) __bf16 bf16x8;
typedef __attribute__((ext_vector_type(4))) short s16x4;
typedef __attribute__((ext_vector_type(4))) float f32x4;
typedef __attribute__((ext_vector_type(4))) unsigned u32x4;
typedef __attribute__((ext_vector_type(2))) unsigned u32x2;
typedef unsigned short u16;

#define S_TOK 2048
#define D_MODEL 512
#define N_HEAD 32
#define F_DIM 1792

#define LDS_AS __attribute__((address_space(3)))

__device__ __forceinline__ float bf2f(unsigned b) { return __uint_as_float(b << 16); }
__device__ __forceinline__ unsigned f2bf(float f) {
  __bf16 h = (__bf16)f;
  return (unsigned)__builtin_bit_cast(unsigned short, h);
}
__device__ __forceinline__ float rbf(float f) { return bf2f(f2bf(f)); }
__device__ __forceinline__ unsigned pack2(float lo, float hi) { return f2bf(lo) | (f2bf(hi) << 16); }

__device__ __forceinline__ f32x4 mfma32(bf16x8 a, bf16x8 b, f32x4 c) {
  return __builtin_amdgcn_mfma_f32_16x16x32_bf16(a, b, c, 0, 0, 0);
}
__device__ __forceinline__ f32x4 mfma16(s16x4 a, s16x4 b, f32x4 c) {
  return __builtin_amdgcn_mfma_f32_16x16x16bf16_1k(a, b, c, 0, 0, 0);
}
__device__ __forceinline__ s16x4 tr_read(const char* p) {
  return __builtin_amdgcn_ds_read_tr16_b64_v4i16((LDS_AS s16x4*)(p));
}
__device__ __forceinline__ void glds16(const void* g, char* l) {
  __builtin_amdgcn_global_load_lds((const __attribute__((address_space(1))) unsigned*)g, (LDS_AS unsigned*)l, 16, 0, 0);
}
__device__ __forceinline__ void glds16a(const void* gsrc, unsigned lds_dst) {
  unsigned keep;
  asm volatile("s_mov_b32 %0, m0\n\ts_mov_b32 m0, %2\n\ts_nop 0\n\tglobal_load_lds_dwordx4 %1, off\n\ts_mov_b32 m0, %0"
               : "=&s"(keep) : "v"(gsrc), "s"(lds_dst) : "memory");
}
__device__ __forceinline__ unsigned lds_addr(const void* p) { return (unsigned)(uintptr_t)(LDS_AS const char*)p; }
#define WAIT_VM(n) asm volatile("s_waitcnt vmcnt(" #n ")" ::: "memory")
#define RAW_BARRIER() do { asm volatile("s_waitcnt lgkmcnt(0)" ::: "memory"); __builtin_amdgcn_s_barrier(); } while (0)

__device__ __forceinline__ float wsum_16_32(float v) {
  v += __shfl_xor(v, 16);
  v += __shfl_xor(v, 32);
  return v;
}
__device__ __forceinline__ float wmax_16_32(float v) {
  v = fmaxf(v, __shfl_xor(v, 16));
  v = fmaxf(v, __shfl_xor(v, 32));
  return v;
}
template <int CTRL> __device__ __forceinline__ float dppf(float v) {
  return __int_as_float(__builtin_amdgcn_update_dpp(0, __float_as_int(v), CTRL, 0xf, 0xf, true));
}
__device__ __forceinline__ float wsum16(float v) {
  v += dppf<0xB1>(v);
  v += dppf<0x4E>(v);
  v += dppf<0x141>(v);
  v += dppf<0x140>(v);
  return v;
}
__device__ __forceinline__ float wmax16(float v) {
  v = fmaxf(v, dppf<0xB1>(v));
  v = fmaxf(v, dppf<0x4E>(v));
  v = fmaxf(v, dppf<0x141>(v));
  v = fmaxf(v, dppf<0x140>(v));
  return v;
}
__device__ __forceinline__ float wsum8(float v) {
  v += dppf<0xB1>(v);
  v += dppf<0x4E>(v);
  v += dppf<0x141>(v);
  return v;
}

__global__ __launch_bounds__(512) void k1_qkv(const u16* __restrict__ x, const u16* __restrict__ wqkv,
                                              const u16* __restrict__ wphi, u16* __restrict__ phiq,
                                              float* __restrict__ kvpart, float* __restrict__ zpart) {
  __shared__ __attribute__((aligned(16))) char smem[163840];
  char* As = smem;
  char* Ws = smem + 65536;
  const int tid = threadIdx.x, wave = tid >> 6, lane = tid & 63;
  const int b = blockIdx.x, xcd = b & 7, idx = b >> 3;
  const int rt = xcd * 2 + (idx >> 4), hp = idx & 15;
  const int row0 = rt * 128;

  u32x4 xr[2][8];
#pragma unroll
  for (int p = 0; p < 2; ++p)
#pragma unroll
    for (int i = 0; i < 8; ++i)
      xr[p][i] = *(const u32x4*)(x + (size_t)(row0 + wave * 16 + p * 8 + (lane >> 3)) * 512 + i * 64 + (lane & 7) * 8);

#pragma unroll
  for (int j = 0; j < 12; ++j) {
    int ii = wave * 12 + j;
    int c = ii * 64 + lane;
    int cqs = c & 3, kr = (c >> 2) & 7, sub = c >> 5;
    int nb = sub % 3, kb = sub / 3;
    int cq = cqs ^ ((kb & 1) << 1);
    const u16* g = wqkv + (size_t)(kb * 8 + kr) * 1536 + nb * 512 + hp * 32 + cq * 8;
    glds16(g, Ws + ii * 1024);
  }
  const int g4 = lane >> 4, l16 = lane & 15;
  const int wn = wave & 1, wm = wave >> 1;
  const int head = hp * 2 + wn;
  s16x4 wph;
#pragma unroll
  for (int j = 0; j < 4; ++j) wph[j] = (short)wphi[head * 256 + (g4 * 4 + j) * 16 + l16];

  float mean[2], rden[2];
#pragma unroll
  for (int p = 0; p < 2; ++p) {
    float s = 0.f;
#pragma unroll
    for (int i = 0; i < 8; ++i)
#pragma unroll
      for (int e = 0; e < 4; ++e) s += bf2f(xr[p][i][e] & 0xffff) + bf2f(xr[p][i][e] >> 16);
    s = wsum8(s);
    float m = s * (1.0f / 512.0f);
    float q = 0.f;
#pragma unroll
    for (int i = 0; i < 8; ++i)
#pragma unroll
      for (int e = 0; e < 4; ++e) {
        float a = bf2f(xr[p][i][e] & 0xffff) - m, c = bf2f(xr[p][i][e] >> 16) - m;
        q += a * a + c * c;
      }
    q = wsum8(q);
    float var = rbf(q * (1.0f / 512.0f));
    float den = rbf(sqrtf(rbf(var + 1e-5f)));
    float ri = 1.0f / den;
    rden[p] = ri;
    mean[p] = -rbf(m) * ri;
  }
  auto write_half = [&](int half) {
#pragma unroll
    for (int p = 0; p < 2; ++p) {
      int r = wave * 16 + p * 8 + (lane >> 3);
#pragma unroll
      for (int i = 0; i < 4; ++i) {
        u32x4 v = xr[p][half * 4 + i], o;
#pragma unroll
        for (int e = 0; e < 4; ++e) {
          float a = fmaf(bf2f(v[e] & 0xffff), rden[p], mean[p]);
          float c = fmaf(__uint_as_float(v[e] & 0xffff0000u), rden[p], mean[p]);
          o[e] = pack2(a, c);
        }
        int chunk = i * 8 + (lane & 7);
        *(u32x4*)(As + r * 512 + ((chunk ^ (r & 15)) << 4)) = o;
      }
    }
  };

  f32x4 acc[2][3];
#pragma unroll
  for (int mi = 0; mi < 2; ++mi)
#pragma unroll
    for (int t = 0; t < 3; ++t) acc[mi][t] = (f32x4){0.f, 0.f, 0.f, 0.f};

  const int tq = l16 >> 2, tp = l16 & 3;
  auto kloop = [&](int half) {
#pragma unroll
    for (int ks = 0; ks < 8; ++ks) {
      bf16x8 af[2], wf[3];
#pragma unroll
      for (int mi = 0; mi < 2; ++mi) {
        int r = wm * 32 + mi * 16 + l16;
        int chunk = ks * 4 + g4;
        af[mi] = *(const bf16x8*)(As + r * 512 + ((chunk ^ (r & 15)) << 4));
      }
      int kb = (half * 8 + ks) * 4 + g4;
#pragma unroll
      for (int t = 0; t < 3; ++t) {
        int cq = (wn * 2 + (tp >> 1)) ^ ((g4 & 1) << 1);
        const char* p0 = Ws + ((kb * 3 + t) * 8 + tq) * 64 + cq * 16 + (tp & 1) * 8;
        s16x4 lo = tr_read(p0), hi = tr_read(p0 + 256);
        u32x4 w = {((unsigned*)&lo)[0], ((unsigned*)&lo)[1], ((unsigned*)&hi)[0], ((unsigned*)&hi)[1]};
        wf[t] = __builtin_bit_cast(bf16x8, w);
      }
#pragma unroll
      for (int mi = 0; mi < 2; ++mi) {
        acc[mi][0] = mfma32(wf[0], af[mi], acc[mi][0]);
        acc[mi][1] = mfma32(wf[1], af[mi], acc[mi][1]);
        acc[mi][2] = mfma32(af[mi], wf[2], acc[mi][2]);
      }
    }
  };

  write_half(0);
  __syncthreads();
  kloop(0);
  __syncthreads();
  write_half(1);
  __syncthreads();
  kloop(1);

  f32x4 kvacc[2] = {{0.f, 0.f, 0.f, 0.f}, {0.f, 0.f, 0.f, 0.f}};
  float zp[2] = {0.f, 0.f};
#pragma unroll
  for (int mi = 0; mi < 2; ++mi) {
    s16x4 qb, kbf, vb;
#pragma unroll
    for (int r = 0; r < 4; ++r) {
      qb[r] = (short)f2bf(acc[mi][0][r]);
      kbf[r] = (short)f2bf(acc[mi][1][r]);
      vb[r] = (short)f2bf(acc[mi][2][r]);
    }
    f32x4 zero = {0.f, 0.f, 0.f, 0.f};
    f32x4 pq = mfma16(wph, qb, zero);
    f32x4 pk = mfma16(kbf, wph, zero);
    {
      float v[4], mx = -3.0e38f, mn = 3.0e38f;
#pragma unroll
      for (int r = 0; r < 4; ++r) { v[r] = rbf(pq[r]); mx = fmaxf(mx, v[r]); mn = fminf(mn, v[r]); }
      mx = wmax_16_32(mx);
      mn = -wmax_16_32(-mn);
      float ep[4], en[4], sp = 0.f, sn = 0.f;
#pragma unroll
      for (int r = 0; r < 4; ++r) { ep[r] = __expf(v[r] - mx); en[r] = __expf(mn - v[r]); sp += ep[r]; sn += en[r]; }
      sp = wsum_16_32(sp);
      sn = wsum_16_32(sn);
      float ip = __builtin_amdgcn_rcpf(sp), in_ = __builtin_amdgcn_rcpf(sn);
      int token = row0 + wm * 32 + mi * 16 + l16;
      u16* dst = phiq + (size_t)token * 1024 + head * 32 + g4 * 4;
      u32x2 o1 = {pack2(ep[0] * ip, ep[1] * ip), pack2(ep[2] * ip, ep[3] * ip)};
      u32x2 o2 = {pack2(en[0] * in_, en[1] * in_), pack2(en[2] * in_, en[3] * in_)};
      *(u32x2*)dst = o1;
      *(u32x2*)(dst + 16) = o2;
    }
    {
      s16x4 php, phn;
#pragma unroll
      for (int r = 0; r < 4; ++r) {
        float v = rbf(pk[r]);
        float mx = wmax16(v), mn = -wmax16(-v);
        float ep = __expf(v - mx), en = __expf(mn - v);
        float sp = wsum16(ep), sn = wsum16(en);
        unsigned a = f2bf(ep * __builtin_amdgcn_rcpf(sp)), c = f2bf(en * __builtin_amdgcn_rcpf(sn));
        php[r] = (short)a;
        phn[r] = (short)c;
        zp[0] += bf2f(a);
        zp[1] += bf2f(c);
      }
      kvacc[0] = mfma16(php, vb, kvacc[0]);
      kvacc[1] = mfma16(phn, vb, kvacc[1]);
    }
  }
  zp[0] = wsum_16_32(zp[0]);
  zp[1] = wsum_16_32(zp[1]);
  RAW_BARRIER();
  float* red = (float*)smem;
  float* redz = red + 8 * 512;
#pragma unroll
  for (int sg = 0; sg < 2; ++sg) *(f32x4*)(red + wave * 512 + sg * 256 + l16 * 16 + g4 * 4) = kvacc[sg];
  if (g4 == 0) { redz[wave * 32 + l16] = zp[0]; redz[wave * 32 + 16 + l16] = zp[1]; }
  RAW_BARRIER();
  {
    int hh = tid >> 8, e = tid & 255;
    int sg = e >> 7, d = (e >> 3) & 15, fq = e & 7;
    float s0 = 0.f, s1 = 0.f;
#pragma unroll
    for (int w = 0; w < 4; ++w) {
      const float* p = red + (w * 2 + hh) * 512 + sg * 256 + d * 16 + fq * 2;
      s0 += p[0];
      s1 += p[1];
    }
    float* dst = kvpart + ((size_t)(rt * 32 + hp * 2 + hh) * 16 + d) * 32 + sg * 16 + fq * 2;
    dst[0] = s0;
    dst[1] = s1;
    if (tid < 64) {
      int h2 = tid >> 5, f = tid & 31;
      float z = 0.f;
#pragma unroll
      for (int w = 0; w < 4; ++w) z += redz[(w * 2 + h2) * 32 + f];
      zpart[(rt * 32 + hp * 2 + h2) * 32 + f] = z;
    }
  }
}

__global__ __launch_bounds__(256) void k2_attn(const u16* __restrict__ phiq, const float* __restrict__ kvpart,
                                               const float* __restrict__ zpart, u16* __restrict__ o,
                                               const u16* __restrict__ wg, const u16* __restrict__ wu,
                                               float* __restrict__ colsum) {
  __shared__ __attribute__((aligned(16))) float sm[2048 + 64];
  const int tid = threadIdx.x, wave = tid >> 6, lane = tid & 63;
  const int b = blockIdx.x;
  if (b >= 256) {
    int j = b - 256;
    const u16* w = (j < 14) ? wg : wu;
    int c0 = (j % 14) * 128;
    int ch = tid & 15, rg = tid >> 4;
    float s[8] = {0.f, 0.f, 0.f, 0.f, 0.f, 0.f, 0.f, 0.f};
#pragma unroll 8
    for (int r = 0; r < 32; ++r) {
      u32x4 v = *(const u32x4*)(w + (size_t)(rg * 32 + r) * F_DIM + c0 + ch * 8);
#pragma unroll
      for (int e = 0; e < 4; ++e) { s[e * 2] += bf2f(v[e] & 0xffff); s[e * 2 + 1] += bf2f(v[e] >> 16); }
    }
#pragma unroll
    for (int e = 0; e < 8; ++e) sm[rg * 128 + ch * 8 + e] = s[e];
    __syncthreads();
    if (tid < 128) {
      float t = 0.f;
#pragma unroll
      for (int r = 0; r < 16; ++r) t += sm[r * 128 + tid];
      colsum[(j < 14 ? 0 : F_DIM) + c0 + tid] = t;
    }
    return;
  }
  const int xcd = b & 7, idx = b >> 3;
  const int rt = xcd * 2 + (idx >> 4), hp = idx & 15;
  const int row0 = rt * 128;
  u16* kvs = (u16*)sm;
  u16* zs = kvs + 1024;
  {
    int e = tid * 4;
    int hh = e >> 9, r = e & 511;
    f32x4 s = {0.f, 0.f, 0.f, 0.f};
#pragma unroll
    for (int p = 0; p < 16; ++p) s += *(const f32x4*)(kvpart + (size_t)(p * 32 + hp * 2 + hh) * 512 + r);
    u32x2 v = {pack2(s[0], s[1]), pack2(s[2], s[3])};
    *(u32x2*)(kvs + e) = v;
    if (tid < 64) {
      float z = 0.f;
#pragma unroll
      for (int p = 0; p < 16; ++p) z += zpart[(p * 32 + hp * 2 + (tid >> 5)) * 32 + (tid & 31)];
      zs[tid] = (u16)f2bf(z);
    }
  }
  __syncthreads();
  const int g4 = lane >> 4, l16 = lane & 15;
#pragma unroll
  for (int hh = 0; hh < 2; ++hh) {
    bf16x8 akv = *(const bf16x8*)(kvs + hh * 512 + l16 * 32 + g4 * 8);
    bf16x8 az = *(const bf16x8*)(zs + hh * 32 + g4 * 8);
    int head = hp * 2 + hh;
#pragma unroll
    for (int mi = 0; mi < 2; ++mi) {
      int token = row0 + wave * 32 + mi * 16 + l16;
      bf16x8 pf = *(const bf16x8*)(phiq + (size_t)token * 1024 + head * 32 + g4 * 8);
      f32x4 zero = {0.f, 0.f, 0.f, 0.f};
      f32x4 num = mfma32(akv, pf, zero);
      f32x4 den = mfma32(az, pf, zero);
      float ov[4];
#pragma unroll
      for (int r = 0; r < 4; ++r) {
        float dn = rbf(rbf(den[r]) + 1e-6f);
        ov[r] = rbf(num[r]) / dn;
      }
      u32x2 v = {pack2(ov[0], ov[1]), pack2(ov[2], ov[3])};
      *(u32x2*)(o + (size_t)token * 512 + head * 16 + g4 * 4) = v;
    }
  }
}

struct GemmArgs {
  const u16* A; const u16* W; const u16* W2; const u16* resid; u16* out; float* stats; const float* colsum;
};

template <int BM, int BN, int WM, int WN, int NK, int NST, int MODE, int NLW>
__global__ __launch_bounds__((WM * WN + NLW) * 64) void gemm_k(GemmArgs args) {
  constexpr int NW = WM * WN, WTM = BM / WM, WTN = BN / WN, MT = WTM / 16, NT = WTN / 16;
  constexpr int ASZ = BM * 128, WSZ = BN * 128, STG = ASZ + WSZ;
  constexpr int APER = BM / 8 / NLW, WPER = BN / 8 / NLW, PER = APER + WPER;
  constexpr int LDA = (MODE == 5) ? F_DIM : 512;
  constexpr int LDW = (MODE == 4) ? F_DIM : 512;
  constexpr int NCT = (MODE == 4) ? (F_DIM / (BN / 2)) : (512 / BN);
  constexpr int NRT = S_TOK / BM, RPX = NRT / 8;
  __shared__ __attribute__((aligned(16))) char smem[NST * STG + ((MODE == 4) ? BM * 8 + 1024 : 0)];
  const int tid = threadIdx.x, wave = tid >> 6, lane = tid & 63;
  const int b = blockIdx.x, xcd = b & 7, idx = b >> 3;
  const int rt = xcd * RPX + idx / NCT, ct = idx % NCT;
  const int row0 = rt * BM;
  const int g4 = lane >> 4, l16 = lane & 15;
  const int wm = wave / WN, wn = wave % WN;

  auto wswz = [](int k) { return (BN == 64) ? (((k >> 1) & 1) | (((k >> 3) & 1) << 1)) : ((k & 3) | (((k >> 3) & 1) << 2)); };
  const unsigned sbase = lds_addr(smem);
  const int lw = __builtin_amdgcn_readfirstlane(wave) - NW;
  const u16* asrc[APER];
  const u16* wsrc[WPER];
#pragma unroll
  for (int j = 0; j < APER; ++j) {
    int ii = lw * APER + j;
    int r = ii * 8 + (lane >> 3);
    int chunk = (lane & 7) ^ (r & 7);
    asrc[j] = args.A + (size_t)(row0 + r) * LDA + chunk * 8;
  }
#pragma unroll
  for (int j = 0; j < WPER; ++j) {
    int ii = lw * WPER + j;
    int c = ii * 64 + lane;
    int k = c / (BN / 8), pos = c % (BN / 8);
    int grp = (pos >> 1) ^ wswz(k), col = grp * 16 + (pos & 1) * 8;
    if constexpr (MODE == 4) {
      const u16* base = (col >= BN / 2) ? args.W2 : args.W;
      wsrc[j] = base + (size_t)k * LDW + ct * (BN / 2) + (col & (BN / 2 - 1));
    } else {
      wsrc[j] = args.W + (size_t)k * LDW + ct * BN + col;
    }
  }
  auto issue = [&](int kt) {
    unsigned st = sbase + (kt % NST) * STG;
#pragma unroll
    for (int j = 0; j < APER; ++j) glds16a(asrc[j] + kt * 64, st + (lw * APER + j) * 1024);
#pragma unroll
    for (int j = 0; j < WPER; ++j) glds16a(wsrc[j] + (size_t)kt * 64 * LDW, st + ASZ + (lw * WPER + j) * 1024);
  };

  if (lw >= 0) {
    if constexpr (MODE == 4) {
      if (lw == 0) glds16a(args.colsum + ((lane >> 5) ? F_DIM : 0) + ct * (BN / 2) + (lane & 31) * 4, sbase + NST * STG + BM * 8);
    }
#pragma unroll
    for (int s = 0; s < NST - 1; ++s)
      if (s < NK) issue(s);
    for (int kt = 0; kt < NK; ++kt) {
      if (NK - 1 - kt >= NST - 2) {
        asm volatile("s_waitcnt vmcnt(%0)" ::"n"(PER * (NST - 2)) : "memory");
      } else {
        asm volatile("s_waitcnt vmcnt(0)" ::: "memory");
      }
      RAW_BARRIER();
      if (kt + NST - 1 < NK) issue(kt + NST - 1);
    }
    return;
  }
  f32x4 acc[MT][NT];
#pragma unroll
  for (int mi = 0; mi < MT; ++mi)
#pragma unroll
    for (int ni = 0; ni < NT; ++ni) acc[mi][ni] = (f32x4){0.f, 0.f, 0.f, 0.f};

  u32x2 res[(MODE == 4) ? 1 : MT][(MODE == 4) ? 1 : NT];
  if constexpr (MODE != 4) {
#pragma unroll
    for (int mi = 0; mi < MT; ++mi)
#pragma unroll
      for (int ni = 0; ni < NT; ++ni)
        res[mi][ni] = *(const u32x2*)(args.resid + (size_t)(row0 + wm * WTM + mi * 16 + l16) * 512 + ct * BN + wn * WTN + ni * 16 + g4 * 4);
  } else {
  }
  if constexpr (MODE == 4) {
      constexpr int NPART = 16;
      const int lt = tid;
      if (lt < BM) {
        const float2* sp = (const float2*)(args.stats) + (size_t)(row0 + lt) * NPART;
        float s1 = 0.f;
        float2 pv[NPART];
#pragma unroll
        for (int p = 0; p < NPART; ++p) { pv[p] = sp[p]; s1 += pv[p].x; }
        float mean = s1 * (1.0f / 512.0f);
        float m2 = 0.f;
#pragma unroll
        for (int p = 0; p < NPART; ++p) { float d = pv[p].x * (1.0f / 32.0f) - mean; m2 += pv[p].y + 32.0f * d * d; }
        float var = rbf(m2 * (1.0f / 512.0f));
        float den = rbf(sqrtf(rbf(var + 1e-5f)));
        float* rs = (float*)(smem + NST * STG);
        rs[lt * 2] = rbf(mean);
        rs[lt * 2 + 1] = 1.0f / den;
      }
    }
  const int tq = l16 >> 2, tp = l16 & 3;
  bf16x8 fa[2][MT], fw[2][NT];
  auto load_frags = [&](int kt, int ks, bf16x8* af, bf16x8* wf) {
    const char* st = smem + (kt % NST) * STG;
#pragma unroll
    for (int mi = 0; mi < MT; ++mi) {
      int r = wm * WTM + mi * 16 + l16;
      af[mi] = *(const bf16x8*)(st + r * 128 + (((ks * 4 + g4) ^ (r & 7)) << 4));
    }
#pragma unroll
    for (int ni = 0; ni < NT; ++ni) {
      int n = (MODE == 4) ? ((ni < NT / 2) ? wn * (WTN / 2) + ni * 16 : BN / 2 + wn * (WTN / 2) + (ni - NT / 2) * 16)
                          : wn * WTN + ni * 16;
      int k0 = ks * 32 + g4 * 8 + tq, k1 = k0 + 4;
      const char* p0 = st + ASZ + k0 * (BN * 2) + (((n >> 4) ^ wswz(k0)) << 5) + tp * 8;
      const char* p1 = st + ASZ + k1 * (BN * 2) + (((n >> 4) ^ wswz(k1)) << 5) + tp * 8;
      s16x4 lo = tr_read(p0), hi = tr_read(p1);
      u32x4 w = {((unsigned*)&lo)[0], ((unsigned*)&lo)[1], ((unsigned*)&hi)[0], ((unsigned*)&hi)[1]};
      wf[ni] = __builtin_bit_cast(bf16x8, w);
    }
  };
  auto mma = [&](const bf16x8* af, const bf16x8* wf) {
#pragma unroll
    for (int mi = 0; mi < MT; ++mi)
#pragma unroll
      for (int ni = 0; ni < NT; ++ni) acc[mi][ni] = mfma32(wf[ni], af[mi], acc[mi][ni]);
  };
  auto stage_sync = [&](int kt) { RAW_BARRIER(); };
  if constexpr (MODE != 4) {
    stage_sync(0);
    load_frags(0, 0, fa[0], fw[0]);
    for (int kt = 0; kt < NK; ++kt) {
      load_frags(kt, 1, fa[1], fw[1]);
      mma(fa[0], fw[0]);
      if (kt + 1 < NK) {
        stage_sync(kt + 1);
        load_frags(kt + 1, 0, fa[0], fw[0]);
      }
      mma(fa[1], fw[1]);
    }
  } else {
    for (int kt = 0; kt < NK; ++kt) {
      stage_sync(kt);
      load_frags(kt, 0, fa[0], fw[0]);
      mma(fa[0], fw[0]);
      load_frags(kt, 1, fa[0], fw[0]);
      mma(fa[0], fw[0]);
    }
  }

  if constexpr (MODE == 3) {
#pragma unroll
    for (int mi = 0; mi < MT; ++mi) {
      int token = row0 + wm * WTM + mi * 16 + l16;
      float hv[NT][4];
      float s1 = 0.f;
#pragma unroll
      for (int ni = 0; ni < NT; ++ni) {
        int n = ct * BN + wn * WTN + ni * 16 + g4 * 4;
        u32x2 xv = res[mi][ni];
        hv[ni][0] = rbf(bf2f(xv[0] & 0xffff) + rbf(acc[mi][ni][0]));
        hv[ni][1] = rbf(bf2f(xv[0] >> 16) + rbf(acc[mi][ni][1]));
        hv[ni][2] = rbf(bf2f(xv[1] & 0xffff) + rbf(acc[mi][ni][2]));
        hv[ni][3] = rbf(bf2f(xv[1] >> 16) + rbf(acc[mi][ni][3]));
        u32x2 ov = {pack2(hv[ni][0], hv[ni][1]), pack2(hv[ni][2], hv[ni][3])};
        *(u32x2*)(args.out + (size_t)token * 512 + n) = ov;
        s1 += hv[ni][0] + hv[ni][1] + hv[ni][2] + hv[ni][3];
      }
      s1 = wsum_16_32(s1);
      float mloc = s1 * (1.0f / (float)WTN);
      float m2 = 0.f;
#pragma unroll
      for (int ni = 0; ni < NT; ++ni)
#pragma unroll
        for (int r = 0; r < 4; ++r) { float d = hv[ni][r] - mloc; m2 += d * d; }
      m2 = wsum_16_32(m2);
      if (g4 == 0) {
        float2 sv = make_float2(s1, m2);
        *(float2*)(args.stats + ((size_t)token * (512 / WTN) + (ct * BN + wn * WTN) / WTN) * 2) = sv;
      }
    }
  } else if constexpr (MODE == 4) {
    const float* rs = (const float*)(smem + NST * STG);
#pragma unroll
    for (int mi = 0; mi < MT; ++mi) {
      int tl = wm * WTM + mi * 16 + l16;
      float mean = rs[tl * 2], rstd = rs[tl * 2 + 1];
      int token = row0 + tl;
#pragma unroll
      for (int ni = 0; ni < NT / 2; ++ni) {
        int f = ct * (BN / 2) + wn * 32 + ni * 16 + g4 * 4;
        const float* csl = (const float*)(smem + NST * STG + BM * 8) + wn * 32 + ni * 16 + g4 * 4;
        f32x4 cg = *(const f32x4*)csl, cu = *(const f32x4*)(csl + 128);
        float ov[4];
#pragma unroll
        for (int r = 0; r < 4; ++r) {
          float gv = rbf((acc[mi][ni][r] - mean * cg[r]) * rstd);
          float uv = rbf((acc[mi][ni + NT / 2][r] - mean * cu[r]) * rstd);
          float sg = rbf(gv * __builtin_amdgcn_rcpf(1.0f + __expf(-gv)));
          ov[r] = sg * uv;
        }
        u32x2 o2 = {pack2(ov[0], ov[1]), pack2(ov[2], ov[3])};
        *(u32x2*)(args.out + (size_t)token * F_DIM + f) = o2;
      }
    }
  } else {
#pragma unroll
    for (int mi = 0; mi < MT; ++mi) {
      int token = row0 + wm * WTM + mi * 16 + l16;
#pragma unroll
      for (int ni = 0; ni < NT; ++ni) {
        int n = ct * BN + wn * WTN + ni * 16 + g4 * 4;
        u32x2 hv = res[mi][ni];
        float o0 = bf2f(hv[0] & 0xffff) + rbf(acc[mi][ni][0]);
        float o1 = bf2f(hv[0] >> 16) + rbf(acc[mi][ni][1]);
        float o2 = bf2f(hv[1] & 0xffff) + rbf(acc[mi][ni][2]);
        float o3 = bf2f(hv[1] >> 16) + rbf(acc[mi][ni][3]);
        u32x2 ov = {pack2(o0, o1), pack2(o2, o3)};
        *(u32x2*)(args.out + (size_t)token * 512 + n) = ov;
      }
    }
  }
}

extern "C" void kernel_launch(void* const* d_in, const int* in_sizes, int n_in, void* d_out, int out_size, void* d_ws,
                              size_t ws_size, hipStream_t stream) {
  const u16* x = (const u16*)d_in[0];
  const u16* wqkv = (const u16*)d_in[1];
  const u16* wphi = (const u16*)d_in[2];
  const u16* wo = (const u16*)d_in[3];
  const u16* wg = (const u16*)d_in[5];
  const u16* wu = (const u16*)d_in[6];
  const u16* wd = (const u16*)d_in[7];
  u16* out = (u16*)d_out;

  char* ws = (char*)d_ws;
  u16* phiq = (u16*)(ws);
  float* kvpart = (float*)(ws + (4u << 20));
  float* zpart = (float*)(ws + (5u << 20));
  float* colsum = (float*)(ws + (5u << 20) + 65536);
  float* stats = (float*)(ws + (5u << 20) + 131072);
  u16* o = (u16*)(ws + (6u << 20));
  u16* h = (u16*)(ws + (8u << 20));
  u16* act = (u16*)(ws + (10u << 20));

  k1_qkv<<<256, 512, 0, stream>>>(x, wqkv, wphi, phiq, kvpart, zpart);
  k2_attn<<<256 + 28, 256, 0, stream>>>(phiq, kvpart, zpart, o, wg, wu, colsum);
  GemmArgs a3 = {o, wo, nullptr, x, h, stats, nullptr};
  gemm_k<64, 64, 2, 2, 8, 8, 3, 4><<<256, 512, 0, stream>>>(a3);
  GemmArgs a4 = {h, wg, wu, nullptr, act, stats, colsum};
  gemm_k<128, 256, 2, 4, 8, 3, 4, 4><<<224, 768, 0, stream>>>(a4);
  GemmArgs a5 = {act, wd, nullptr, h, out, nullptr, nullptr};
  gemm_k<64, 64, 2, 2, 28, 8, 5, 4><<<256, 512, 0, stream>>>(a5);
}
```

```cpp
#include <hip/hip_runtime.h>
#include <hip/hip_bf16.h>
#include <stdint.h>


typedef __attribute__((ext_vector_type(8))) __bf16 bf16x8;
typedef __attribute__((ext_vector_type(4))) short s16x4;
typedef __attribute__((ext_vector_type(4))) float f32x4;
typedef __attribute__((ext_vector_type(4))) unsigned u32x4;
typedef __attribute__((ext_vector_type(2))) unsigned u32x2;
typedef unsigned short u16;

#define S_TOK 2048
#define D_MODEL 512
#define N_HEAD 32
#define F_DIM 1792

#define LDS_AS __attribute__((address_space(3)))

__device__ __forceinline__ float bf2f(unsigned b) { return __uint_as_float(b << 16); }
__device__ __forceinline__ unsigned f2bf(float f) {
  __bf16 h = (__bf16)f;
  return (unsigned)__builtin_bit_cast(unsigned short, h);
}
__device__ __forceinline__ float rbf(float f) { return bf2f(f2bf(f)); }
__device__ __forceinline__ unsigned pack2(float lo, float hi) { return f2bf(lo) | (f2bf(hi) << 16); }

__device__ __forceinline__ f32x4 mfma32(bf16x8 a, bf16x8 b, f32x4 c) {
  return __builtin_amdgcn_mfma_f32_16x16x32_bf16(a, b, c, 0, 0, 0);
}
__device__ __forceinline__ f32x4 mfma16(s16x4 a, s16x4 b, f32x4 c) {
  return __builtin_amdgcn_mfma_f32_16x16x16bf16_1k(a, b, c, 0, 0, 0);
}
__device__ __forceinline__ s16x4 tr_read(const char* p) {
  return __builtin_amdgcn_ds_read_tr16_b64_v4i16((LDS_AS s16x4*)(p));
}
__device__ __forceinline__ void glds16(const void* g, char* l) {
  __builtin_amdgcn_global_load_lds((const __attribute__((address_space(1))) unsigned*)g, (LDS_AS unsigned*)l, 16, 0, 0);
}
__device__ __forceinline__ void glds16a(const void* gsrc, unsigned lds_dst) {
  unsigned keep;
  asm volatile("s_mov_b32 %0, m0\n\ts_mov_b32 m0, %2\n\ts_nop 0\n\tglobal_load_lds_dwordx4 %1, off\n\ts_mov_b32 m0, %0"
               : "=&s"(keep) : "v"(gsrc), "s"(lds_dst) : "memory");
}
__device__ __forceinline__ unsigned lds_addr(const void* p) { return (unsigned)(uintptr_t)(LDS_AS const char*)p; }
#define WAIT_VM(n) asm volatile("s_waitcnt vmcnt(" #n ")" ::: "memory")
#define RAW_BARRIER() do { asm volatile("s_waitcnt lgkmcnt(0)" ::: "memory"); __builtin_amdgcn_s_barrier(); } while (0)

__device__ __forceinline__ float wsum_16_32(float v) {
  v += __shfl_xor(v, 16);
  v += __shfl_xor(v, 32);
  return v;
}
__device__ __forceinline__ float wmax_16_32(float v) {
  v = fmaxf(v, __shfl_xor(v, 16));
  v = fmaxf(v, __shfl_xor(v, 32));
  return v;
}
template <int CTRL> __device__ __forceinline__ float dppf(float v) {
  return __int_as_float(__builtin_amdgcn_update_dpp(0, __float_as_int(v), CTRL, 0xf, 0xf, true));
}
__device__ __forceinline__ float wsum16(float v) {
  v += dppf<0xB1>(v);
  v += dppf<0x4E>(v);
  v += dppf<0x141>(v);
  v += dppf<0x140>(v);
  return v;
}
__device__ __forceinline__ float wmax16(float v) {
  v = fmaxf(v, dppf<0xB1>(v));
  v = fmaxf(v, dppf<0x4E>(v));
  v = fmaxf(v, dppf<0x141>(v));
  v = fmaxf(v, dppf<0x140>(v));
  return v;
}
__device__ __forceinline__ float wsum8(float v) {
  v += dppf<0xB1>(v);
  v += dppf<0x4E>(v);
  v += dppf<0x141>(v);
  return v;
}

__global__ __launch_bounds__(512) void k1_qkv(const u16* __restrict__ x, const u16* __restrict__ wqkv,
                                              const u16* __restrict__ wphi, u16* __restrict__ phiq,
                                              float* __restrict__ kvpart, float* __restrict__ zpart) {
  __shared__ __attribute__((aligned(16))) char smem[163840];
  char* As = smem;
  char* Ws = smem + 65536;
  const int tid = threadIdx.x, wave = tid >> 6, lane = tid & 63;
  const int b = blockIdx.x, xcd = b & 7, idx = b >> 3;
  const int rt = xcd * 2 + (idx >> 4), hp = idx & 15;
  const int row0 = rt * 128;

  u32x4 xr[2][8];
#pragma unroll
  for (int p = 0; p < 2; ++p)
#pragma unroll
    for (int i = 0; i < 8; ++i)
      xr[p][i] = *(const u32x4*)(x + (size_t)(row0 + wave * 16 + p * 8 + (lane >> 3)) * 512 + i * 64 + (lane & 7) * 8);

#pragma unroll
  for (int j = 0; j < 12; ++j) {
    int ii = wave * 12 + j;
    int c = ii * 64 + lane;
    int cqs = c & 3, kr = (c >> 2) & 7, sub = c >> 5;
    int nb = sub % 3, kb = sub / 3;
    int cq = cqs ^ ((kb & 1) << 1);
    const u16* g = wqkv + (size_t)(kb * 8 + kr) * 1536 + nb * 512 + hp * 32 + cq * 8;
    glds16(g, Ws + ii * 1024);
  }
  const int g4 = lane >> 4, l16 = lane & 15;
  const int wn = wave & 1, wm = wave >> 1;
  const int head = hp * 2 + wn;
  s16x4 wph;
#pragma unroll
  for (int j = 0; j < 4; ++j) wph[j] = (short)wphi[head * 256 + (g4 * 4 + j) * 16 + l16];

  float mean[2], rden[2];
#pragma unroll
  for (int p = 0; p < 2; ++p) {
    float s = 0.f;
#pragma unroll
    for (int i = 0; i < 8; ++i)
#pragma unroll
      for (int e = 0; e < 4; ++e) s += bf2f(xr[p][i][e] & 0xffff) + bf2f(xr[p][i][e] >> 16);
    s = wsum8(s);
    float m = s * (1.0f / 512.0f);
    float q = 0.f;
#pragma unroll
    for (int i = 0; i < 8; ++i)
#pragma unroll
      for (int e = 0; e < 4; ++e) {
        float a = bf2f(xr[p][i][e] & 0xffff) - m, c = bf2f(xr[p][i][e] >> 16) - m;
        q += a * a + c * c;
      }
    q = wsum8(q);
    float var = rbf(q * (1.0f / 512.0f));
    float den = rbf(sqrtf(rbf(var + 1e-5f)));
    float ri = 1.0f / den;
    rden[p] = ri;
    mean[p] = -rbf(m) * ri;
  }
  auto write_half = [&](int half) {
#pragma unroll
    for (int p = 0; p < 2; ++p) {
      int r = wave * 16 + p * 8 + (lane >> 3);
#pragma unroll
      for (int i = 0; i < 4; ++i) {
        u32x4 v = xr[p][half * 4 + i], o;
#pragma unroll
        for (int e = 0; e < 4; ++e) {
          float a = fmaf(bf2f(v[e] & 0xffff), rden[p], mean[p]);
          float c = fmaf(__uint_as_float(v[e] & 0xffff0000u), rden[p], mean[p]);
          o[e] = pack2(a, c);
        }
        int chunk = i * 8 + (lane & 7);
        *(u32x4*)(As + r * 512 + ((chunk ^ (r & 15)) << 4)) = o;
      }
    }
  };

  f32x4 acc[2][3];
#pragma unroll
  for (int mi = 0; mi < 2; ++mi)
#pragma unroll
    for (int t = 0; t < 3; ++t) acc[mi][t] = (f32x4){0.f, 0.f, 0.f, 0.f};

  const int tq = l16 >> 2, tp = l16 & 3;
  auto kloop = [&](int half) {
#pragma unroll
    for (int ks = 0; ks < 8; ++ks) {
      bf16x8 af[2], wf[3];
#pragma unroll
      for (int mi = 0; mi < 2; ++mi) {
        int r = wm * 32 + mi * 16 + l16;
        int chunk = ks * 4 + g4;
        af[mi] = *(const bf16x8*)(As + r * 512 + ((chunk ^ (r & 15)) << 4));
      }
      int kb = (half * 8 + ks) * 4 + g4;
#pragma unroll
      for (int t = 0; t < 3; ++t) {
        int cq = (wn * 2 + (tp >> 1)) ^ ((g4 & 1) << 1);
        const char* p0 = Ws + ((kb * 3 + t) * 8 + tq) * 64 + cq * 16 + (tp & 1) * 8;
        s16x4 lo = tr_read(p0), hi = tr_read(p0 + 256);
        u32x4 w = {((unsigned*)&lo)[0], ((unsigned*)&lo)[1], ((unsigned*)&hi)[0], ((unsigned*)&hi)[1]};
        wf[t] = __builtin_bit_cast(bf16x8, w);
      }
#pragma unroll
      for (int mi = 0; mi < 2; ++mi) {
        acc[mi][0] = mfma32(wf[0], af[mi], acc[mi][0]);
        acc[mi][1] = mfma32(wf[1], af[mi], acc[mi][1]);
        acc[mi][2] = mfma32(af[mi], wf[2], acc[mi][2]);
      }
    }
  };

  write_half(0);
  __syncthreads();
  kloop(0);
  __syncthreads();
  write_half(1);
  __syncthreads();
  kloop(1);

  f32x4 kvacc[2] = {{0.f, 0.f, 0.f, 0.f}, {0.f, 0.f, 0.f, 0.f}};
  float zp[2] = {0.f, 0.f};
#pragma unroll
  for (int mi = 0; mi < 2; ++mi) {
    s16x4 qb, kbf, vb;
#pragma unroll
    for (int r = 0; r < 4; ++r) {
      qb[r] = (short)f2bf(acc[mi][0][r]);
      kbf[r] = (short)f2bf(acc[mi][1][r]);
      vb[r] = (short)f2bf(acc[mi][2][r]);
    }
    f32x4 zero = {0.f, 0.f, 0.f, 0.f};
    f32x4 pq = mfma16(wph, qb, zero);
    f32x4 pk = mfma16(kbf, wph, zero);
    {
      float v[4], mx = -3.0e38f, mn = 3.0e38f;
#pragma unroll
      for (int r = 0; r < 4; ++r) { v[r] = rbf(pq[r]); mx = fmaxf(mx, v[r]); mn = fminf(mn, v[r]); }
      mx = wmax_16_32(mx);
      mn = -wmax_16_32(-mn);
      float ep[4], en[4], sp = 0.f, sn = 0.f;
#pragma unroll
      for (int r = 0; r < 4; ++r) { ep[r] = __expf(v[r] - mx); en[r] = __expf(mn - v[r]); sp += ep[r]; sn += en[r]; }
      sp = wsum_16_32(sp);
      sn = wsum_16_32(sn);
      float ip = __builtin_amdgcn_rcpf(sp), in_ = __builtin_amdgcn_rcpf(sn);
      int token = row0 + wm * 32 + mi * 16 + l16;
      u16* dst = phiq + (size_t)token * 1024 + head * 32 + g4 * 4;
      u32x2 o1 = {pack2(ep[0] * ip, ep[1] * ip), pack2(ep[2] * ip, ep[3] * ip)};
      u32x2 o2 = {pack2(en[0] * in_, en[1] * in_), pack2(en[2] * in_, en[3] * in_)};
      *(u32x2*)dst = o1;
      *(u32x2*)(dst + 16) = o2;
    }
    {
      s16x4 php, phn;
#pragma unroll
      for (int r = 0; r < 4; ++r) {
        float v = rbf(pk[r]);
        float mx = wmax16(v), mn = -wmax16(-v);
        float ep = __expf(v - mx), en = __expf(mn - v);
        float sp = wsum16(ep), sn = wsum16(en);
        unsigned a = f2bf(ep * __builtin_amdgcn_rcpf(sp)), c = f2bf(en * __builtin_amdgcn_rcpf(sn));
        php[r] = (short)a;
        phn[r] = (short)c;
        zp[0] += bf2f(a);
        zp[1] += bf2f(c);
      }
      kvacc[0] = mfma16(php, vb, kvacc[0]);
      kvacc[1] = mfma16(phn, vb, kvacc[1]);
    }
  }
  zp[0] = wsum_16_32(zp[0]);
  zp[1] = wsum_16_32(zp[1]);
  RAW_BARRIER();
  float* red = (float*)smem;
  float* redz = red + 8 * 512;
#pragma unroll
  for (int sg = 0; sg < 2; ++sg) *(f32x4*)(red + wave * 512 + sg * 256 + l16 * 16 + g4 * 4) = kvacc[sg];
  if (g4 == 0) { redz[wave * 32 + l16] = zp[0]; redz[wave * 32 + 16 + l16] = zp[1]; }
  RAW_BARRIER();
  {
    int hh = tid >> 8, e = tid & 255;
    int sg = e >> 7, d = (e >> 3) & 15, fq = e & 7;
    float s0 = 0.f, s1 = 0.f;
#pragma unroll
    for (int w = 0; w < 4; ++w) {
      const float* p = red + (w * 2 + hh) * 512 + sg * 256 + d * 16 + fq * 2;
      s0 += p[0];
      s1 += p[1];
    }
    float* dst = kvpart + ((size_t)(rt * 32 + hp * 2 + hh) * 16 + d) * 32 + sg * 16 + fq * 2;
    dst[0] = s0;
    dst[1] = s1;
    if (tid < 64) {
      int h2 = tid >> 5, f = tid & 31;
      float z = 0.f;
#pragma unroll
      for (int w = 0; w < 4; ++w) z += redz[(w * 2 + h2) * 32 + f];
      zpart[(rt * 32 + hp * 2 + h2) * 32 + f] = z;
    }
  }
}

__global__ __launch_bounds__(256) void k2_attn(const u16* __restrict__ phiq, const float* __restrict__ kvpart,
                                               const float* __restrict__ zpart, u16* __restrict__ o,
                                               const u16* __restrict__ wg, const u16* __restrict__ wu,
                                               float* __restrict__ colsum, const u16* __restrict__ wo,
                                               const u16* __restrict__ wd) {
  __shared__ __attribute__((aligned(16))) float sm[2048 + 64];
  const int tid = threadIdx.x, wave = tid >> 6, lane = tid & 63;
  const int b = blockIdx.x;
  if (b >= 256 + 28) {
    int j = b - (256 + 28);
    const u16* w = (j < 8) ? (wo + (size_t)j * 32768) : (wd + (size_t)(j - 8) * 32768);
#pragma unroll 4
    for (int i = 0; i < 16; ++i) {
      u32x4 v = *(const u32x4*)(w + (size_t)(i * 256 + tid) * 8);
      asm volatile("" ::"v"(v));
    }
    return;
  }
  if (b >= 256) {
    int j = b - 256;
    const u16* w = (j < 14) ? wg : wu;
    int c0 = (j % 14) * 128;
    int ch = tid & 15, rg = tid >> 4;
    float s[8] = {0.f, 0.f, 0.f, 0.f, 0.f, 0.f, 0.f, 0.f};
#pragma unroll 8
    for (int r = 0; r < 32; ++r) {
      u32x4 v = *(const u32x4*)(w + (size_t)(rg * 32 + r) * F_DIM + c0 + ch * 8);
#pragma unroll
      for (int e = 0; e < 4; ++e) { s[e * 2] += bf2f(v[e] & 0xffff); s[e * 2 + 1] += bf2f(v[e] >> 16); }
    }
#pragma unroll
    for (int e = 0; e < 8; ++e) sm[rg * 128 + ch * 8 + e] = s[e];
    __syncthreads();
    if (tid < 128) {
      float t = 0.f;
#pragma unroll
      for (int r = 0; r < 16; ++r) t += sm[r * 128 + tid];
      colsum[(j < 14 ? 0 : F_DIM) + c0 + tid] = t;
    }
    return;
  }
  const int xcd = b & 7, idx = b >> 3;
  const int rt = xcd * 2 + (idx >> 4), hp = idx & 15;
  const int row0 = rt * 128;
  u16* kvs = (u16*)sm;
  u16* zs = kvs + 1024;
  {
    int e = tid * 4;
    int hh = e >> 9, r = e & 511;
    f32x4 s = {0.f, 0.f, 0.f, 0.f};
#pragma unroll
    for (int p = 0; p < 16; ++p) s += *(const f32x4*)(kvpart + (size_t)(p * 32 + hp * 2 + hh) * 512 + r);
    u32x2 v = {pack2(s[0], s[1]), pack2(s[2], s[3])};
    *(u32x2*)(kvs + e) = v;
    if (tid < 64) {
      float z = 0.f;
#pragma unroll
      for (int p = 0; p < 16; ++p) z += zpart[(p * 32 + hp * 2 + (tid >> 5)) * 32 + (tid & 31)];
      zs[tid] = (u16)f2bf(z);
    }
  }
  __syncthreads();
  const int g4 = lane >> 4, l16 = lane & 15;
#pragma unroll
  for (int hh = 0; hh < 2; ++hh) {
    bf16x8 akv = *(const bf16x8*)(kvs + hh * 512 + l16 * 32 + g4 * 8);
    bf16x8 az = *(const bf16x8*)(zs + hh * 32 + g4 * 8);
    int head = hp * 2 + hh;
#pragma unroll
    for (int mi = 0; mi < 2; ++mi) {
      int token = row0 + wave * 32 + mi * 16 + l16;
      bf16x8 pf = *(const bf16x8*)(phiq + (size_t)token * 1024 + head * 32 + g4 * 8);
      f32x4 zero = {0.f, 0.f, 0.f, 0.f};
      f32x4 num = mfma32(akv, pf, zero);
      f32x4 den = mfma32(az, pf, zero);
      float ov[4];
#pragma unroll
      for (int r = 0; r < 4; ++r) {
        float dn = rbf(rbf(den[r]) + 1e-6f);
        ov[r] = rbf(num[r]) / dn;
      }
      u32x2 v = {pack2(ov[0], ov[1]), pack2(ov[2], ov[3])};
      *(u32x2*)(o + (size_t)token * 512 + head * 16 + g4 * 4) = v;
    }
  }
}

struct GemmArgs {
  const u16* A; const u16* W; const u16* W2; const u16* resid; u16* out; float* stats; const float* colsum;
};

template <int BM, int BN, int WM, int WN, int NK, int NST, int MODE, int NLW>
__global__ __launch_bounds__((WM * WN + NLW) * 64) void gemm_k(GemmArgs args) {
  constexpr int NW = WM * WN, WTM = BM / WM, WTN = BN / WN, MT = WTM / 16, NT = WTN / 16;
  constexpr int ASZ = BM * 128, WSZ = BN * 128, STG = ASZ + WSZ;
  constexpr int APER = BM / 8 / NLW, WPER = BN / 8 / NLW, PER = APER + WPER;
  constexpr int LDA = (MODE == 5) ? F_DIM : 512;
  constexpr int LDW = (MODE == 4) ? F_DIM : 512;
  constexpr int NCT = (MODE == 4) ? (F_DIM / (BN / 2)) : (512 / BN);
  constexpr int NRT = S_TOK / BM, RPX = NRT / 8;
  __shared__ __attribute__((aligned(16))) char smem[NST * STG + ((MODE == 4) ? BM * 8 + 1024 : 0)];
  const int tid = threadIdx.x, wave = tid >> 6, lane = tid & 63;
  const int b = blockIdx.x, xcd = b & 7, idx = b >> 3;
  const int rt = xcd * RPX + idx / NCT, ct = idx % NCT;
  const int row0 = rt * BM;
  const int g4 = lane >> 4, l16 = lane & 15;
  const int wm = wave / WN, wn = wave % WN;

  auto wswz = [](int k) { return (BN == 64) ? (((k >> 1) & 1) | (((k >> 3) & 1) << 1)) : ((k & 3) | (((k >> 3) & 1) << 2)); };
  const unsigned sbase = lds_addr(smem);
  const int lw = __builtin_amdgcn_readfirstlane(wave) - NW;
  const u16* asrc[APER];
  const u16* wsrc[WPER];
#pragma unroll
  for (int j = 0; j < APER; ++j) {
    int ii = lw * APER + j;
    int r = ii * 8 + (lane >> 3);
    int chunk = (lane & 7) ^ (r & 7);
    asrc[j] = args.A + (size_t)(row0 + r) * LDA + chunk * 8;
  }
#pragma unroll
  for (int j = 0; j < WPER; ++j) {
    int ii = lw * WPER + j;
    int c = ii * 64 + lane;
    int k = c / (BN / 8), pos = c % (BN / 8);
    int grp = (pos >> 1) ^ wswz(k), col = grp * 16 + (pos & 1) * 8;
    if constexpr (MODE == 4) {
      const u16* base = (col >= BN / 2) ? args.W2 : args.W;
      wsrc[j] = base + (size_t)k * LDW + ct * (BN / 2) + (col & (BN / 2 - 1));
    } else {
      wsrc[j] = args.W + (size_t)k * LDW + ct * BN + col;
    }
  }
  auto issue = [&](int kt) {
    unsigned st = sbase + (kt % NST) * STG;
#pragma unroll
    for (int j = 0; j < APER; ++j) glds16a(asrc[j] + kt * 64, st + (lw * APER + j) * 1024);
#pragma unroll
    for (int j = 0; j < WPER; ++j) glds16a(wsrc[j] + (size_t)kt * 64 * LDW, st + ASZ + (lw * WPER + j) * 1024);
  };

  if (lw >= 0) {
    if constexpr (MODE == 4) {
      if (lw == 0) glds16a(args.colsum + ((lane >> 5) ? F_DIM : 0) + ct * (BN / 2) + (lane & 31) * 4, sbase + NST * STG + BM * 8);
    }
#pragma unroll
    for (int s = 0; s < NST - 1; ++s)
      if (s < NK) issue(s);
    for (int kt = 0; kt < NK; ++kt) {
      if (NK - 1 - kt >= NST - 2) {
        asm volatile("s_waitcnt vmcnt(%0)" ::"n"(PER * (NST - 2)) : "memory");
      } else {
        asm volatile("s_waitcnt vmcnt(0)" ::: "memory");
      }
      RAW_BARRIER();
      if (kt + NST - 1 < NK) issue(kt + NST - 1);
    }
    return;
  }
  f32x4 acc[MT][NT];
#pragma unroll
  for (int mi = 0; mi < MT; ++mi)
#pragma unroll
    for (int ni = 0; ni < NT; ++ni) acc[mi][ni] = (f32x4){0.f, 0.f, 0.f, 0.f};

  u32x2 res[(MODE == 4) ? 1 : MT][(MODE == 4) ? 1 : NT];
  if constexpr (MODE != 4) {
#pragma unroll
    for (int mi = 0; mi < MT; ++mi)
#pragma unroll
      for (int ni = 0; ni < NT; ++ni)
        res[mi][ni] = *(const u32x2*)(args.resid + (size_t)(row0 + wm * WTM + mi * 16 + l16) * 512 + ct * BN + wn * WTN + ni * 16 + g4 * 4);
  } else {
  }
  if constexpr (MODE == 4) {
      constexpr int NPART = 16;
      const int lt = tid;
      if (lt < BM) {
        const float2* sp = (const float2*)(args.stats) + (size_t)(row0 + lt) * NPART;
        float s1 = 0.f;
        float2 pv[NPART];
#pragma unroll
        for (int p = 0; p < NPART; ++p) { pv[p] = sp[p]; s1 += pv[p].x; }
        float mean = s1 * (1.0f / 512.0f);
        float m2 = 0.f;
#pragma unroll
        for (int p = 0; p < NPART; ++p) { float d = pv[p].x * (1.0f / 32.0f) - mean; m2 += pv[p].y + 32.0f * d * d; }
        float var = rbf(m2 * (1.0f / 512.0f));
        float den = rbf(sqrtf(rbf(var + 1e-5f)));
        float* rs = (float*)(smem + NST * STG);
        rs[lt * 2] = rbf(mean);
        rs[lt * 2 + 1] = 1.0f / den;
      }
    }
  const int tq = l16 >> 2, tp = l16 & 3;
  bf16x8 fa[2][MT], fw[2][NT];
  auto load_frags = [&](int kt, int ks, bf16x8* af, bf16x8* wf) {
    const char* st = smem + (kt % NST) * STG;
#pragma unroll
    for (int mi = 0; mi < MT; ++mi) {
      int r = wm * WTM + mi * 16 + l16;
      af[mi] = *(const bf16x8*)(st + r * 128 + (((ks * 4 + g4) ^ (r & 7)) << 4));
    }
#pragma unroll
    for (int ni = 0; ni < NT; ++ni) {
      int n = (MODE == 4) ? ((ni < NT / 2) ? wn * (WTN / 2) + ni * 16 : BN / 2 + wn * (WTN / 2) + (ni - NT / 2) * 16)
                          : wn * WTN + ni * 16;
      int k0 = ks * 32 + g4 * 8 + tq, k1 = k0 + 4;
      const char* p0 = st + ASZ + k0 * (BN * 2) + (((n >> 4) ^ wswz(k0)) << 5) + tp * 8;
      const char* p1 = st + ASZ + k1 * (BN * 2) + (((n >> 4) ^ wswz(k1)) << 5) + tp * 8;
      s16x4 lo = tr_read(p0), hi = tr_read(p1);
      u32x4 w = {((unsigned*)&lo)[0], ((unsigned*)&lo)[1], ((unsigned*)&hi)[0], ((unsigned*)&hi)[1]};
      wf[ni] = __builtin_bit_cast(bf16x8, w);
    }
  };
  auto mma = [&](const bf16x8* af, const bf16x8* wf) {
#pragma unroll
    for (int mi = 0; mi < MT; ++mi)
#pragma unroll
      for (int ni = 0; ni < NT; ++ni) acc[mi][ni] = mfma32(wf[ni], af[mi], acc[mi][ni]);
  };
  auto stage_sync = [&](int kt) { RAW_BARRIER(); };
  if constexpr (MODE != 4) {
    stage_sync(0);
    load_frags(0, 0, fa[0], fw[0]);
    for (int kt = 0; kt < NK; ++kt) {
      load_frags(kt, 1, fa[1], fw[1]);
      mma(fa[0], fw[0]);
      if (kt + 1 < NK) {
        stage_sync(kt + 1);
        load_frags(kt + 1, 0, fa[0], fw[0]);
      }
      mma(fa[1], fw[1]);
    }
  } else {
    for (int kt = 0; kt < NK; ++kt) {
      stage_sync(kt);
      load_frags(kt, 0, fa[0], fw[0]);
      mma(fa[0], fw[0]);
      load_frags(kt, 1, fa[0], fw[0]);
      mma(fa[0], fw[0]);
    }
  }

  if constexpr (MODE == 3) {
#pragma unroll
    for (int mi = 0; mi < MT; ++mi) {
      int token = row0 + wm * WTM + mi * 16 + l16;
      float hv[NT][4];
      float s1 = 0.f;
#pragma unroll
      for (int ni = 0; ni < NT; ++ni) {
        int n = ct * BN + wn * WTN + ni * 16 + g4 * 4;
        u32x2 xv = res[mi][ni];
        hv[ni][0] = rbf(bf2f(xv[0] & 0xffff) + rbf(acc[mi][ni][0]));
        hv[ni][1] = rbf(bf2f(xv[0] >> 16) + rbf(acc[mi][ni][1]));
        hv[ni][2] = rbf(bf2f(xv[1] & 0xffff) + rbf(acc[mi][ni][2]));
        hv[ni][3] = rbf(bf2f(xv[1] >> 16) + rbf(acc[mi][ni][3]));
        u32x2 ov = {pack2(hv[ni][0], hv[ni][1]), pack2(hv[ni][2], hv[ni][3])};
        *(u32x2*)(args.out + (size_t)token * 512 + n) = ov;
        s1 += hv[ni][0] + hv[ni][1] + hv[ni][2] + hv[ni][3];
      }
      s1 = wsum_16_32(s1);
      float mloc = s1 * (1.0f / (float)WTN);
      float m2 = 0.f;
#pragma unroll
      for (int ni = 0; ni < NT; ++ni)
#pragma unroll
        for (int r = 0; r < 4; ++r) { float d = hv[ni][r] - mloc; m2 += d * d; }
      m2 = wsum_16_32(m2);
      if (g4 == 0) {
        float2 sv = make_float2(s1, m2);
        *(float2*)(args.stats + ((size_t)token * (512 / WTN) + (ct * BN + wn * WTN) / WTN) * 2) = sv;
      }
    }
  } else if constexpr (MODE == 4) {
    const float* rs = (const float*)(smem + NST * STG);
#pragma unroll
    for (int mi = 0; mi < MT; ++mi) {
      int tl = wm * WTM + mi * 16 + l16;
      float mean = rs[tl * 2], rstd = rs[tl * 2 + 1];
      int token = row0 + tl;
#pragma unroll
      for (int ni = 0; ni < NT / 2; ++ni) {
        int f = ct * (BN / 2) + wn * 32 + ni * 16 + g4 * 4;
        const float* csl = (const float*)(smem + NST * STG + BM * 8) + wn * 32 + ni * 16 + g4 * 4;
        f32x4 cg = *(const f32x4*)csl, cu = *(const f32x4*)(csl + 128);
        float ov[4];
#pragma unroll
        for (int r = 0; r < 4; ++r) {
          float gv = (acc[mi][ni][r] - mean * cg[r]) * rstd;
          float uv = (acc[mi][ni + NT / 2][r] - mean * cu[r]) * rstd;
          float sg = gv * __builtin_amdgcn_rcpf(1.0f + __expf(-gv));
          ov[r] = sg * uv;
        }
        u32x2 o2 = {pack2(ov[0], ov[1]), pack2(ov[2], ov[3])};
        *(u32x2*)(args.out + (size_t)token * F_DIM + f) = o2;
      }
    }
  } else {
#pragma unroll
    for (int mi = 0; mi < MT; ++mi) {
      int token = row0 + wm * WTM + mi * 16 + l16;
#pragma unroll
      for (int ni = 0; ni < NT; ++ni) {
        int n = ct * BN + wn * WTN + ni * 16 + g4 * 4;
        u32x2 hv = res[mi][ni];
        float o0 = bf2f(hv[0] & 0xffff) + rbf(acc[mi][ni][0]);
        float o1 = bf2f(hv[0] >> 16) + rbf(acc[mi][ni][1]);
        float o2 = bf2f(hv[1] & 0xffff) + rbf(acc[mi][ni][2]);
        float o3 = bf2f(hv[1] >> 16) + rbf(acc[mi][ni][3]);
        u32x2 ov = {pack2(o0, o1), pack2(o2, o3)};
        *(u32x2*)(args.out + (size_t)token * 512 + n) = ov;
      }
    }
  }
}

extern "C" void kernel_launch(void* const* d_in, const int* in_sizes, int n_in, void* d_out, int out_size, void* d_ws,
                              size_t ws_size, hipStream_t stream) {
  const u16* x = (const u16*)d_in[0];
  const u16* wqkv = (const u16*)d_in[1];
  const u16* wphi = (const u16*)d_in[2];
  const u16* wo = (const u16*)d_in[3];
  const u16* wg = (const u16*)d_in[5];
  const u16* wu = (const u16*)d_in[6];
  const u16* wd = (const u16*)d_in[7];
  u16* out = (u16*)d_out;

  char* ws = (char*)d_ws;
  u16* phiq = (u16*)(ws);
  float* kvpart = (float*)(ws + (4u << 20));
  float* zpart = (float*)(ws + (5u << 20));
  float* colsum = (float*)(ws + (5u << 20) + 65536);
  float* stats = (float*)(ws + (5u << 20) + 131072);
  u16* o = (u16*)(ws + (6u << 20));
  u16* h = (u16*)(ws + (8u << 20));
  u16* act = (u16*)(ws + (10u << 20));

  k1_qkv<<<256, 512, 0, stream>>>(x, wqkv, wphi, phiq, kvpart, zpart);
  k2_attn<<<256 + 28 + 36, 256, 0, stream>>>(phiq, kvpart, zpart, o, wg, wu, colsum, wo, wd);
  GemmArgs a3 = {o, wo, nullptr, x, h, stats, nullptr};
  gemm_k<64, 64, 2, 2, 8, 8, 3, 4><<<256, 512, 0, stream>>>(a3);
  GemmArgs a4 = {h, wg, wu, nullptr, act, stats, colsum};
  gemm_k<128, 256, 2, 4, 8, 3, 4, 4><<<224, 768, 0, stream>>>(a4);
  GemmArgs a5 = {act, wd, nullptr, h, out, nullptr, nullptr};
  gemm_k<64, 64, 2, 2, 28, 8, 5, 4><<<256, 512, 0, stream>>>(a5);
}
```
